```python
import torch

def _softmax(x, axis=-1):
    return torch.softmax(x, dim=axis)
def _gelu(x):
    return torch.nn.functional.gelu(x, approximate="tanh")
def _silu(x):
    return torch.nn.functional.silu(x)
def _sigmoid(x):
    return torch.sigmoid(x)
def _layernorm(x, axis=-1, eps=1e-5):
    m = x.mean(dim=axis, keepdim=True)
    v = x.var(dim=axis, keepdim=True, unbiased=False)
    return (x - m) / torch.sqrt(v + eps)
def _scatter_add(tgt, idx, src, axis=0):
    return tgt.clone().index_add_(axis, idx, src)
def _segment_sum(src, seg, n):
    out = torch.zeros((n,) + src.shape[1:], dtype=src.dtype)
    return out.index_add_(0, seg, src)
def _pad(x, pw):
    flat = tuple(v for pair in reversed(pw) for v in pair)
    return torch.nn.functional.pad(x, flat)

kernel_name = 'concat_heads__ssm_conv_s5__softmax_full_mha__prompt'

def setup_inputs():

    _w = (lambda a: (lambda t: t.to(torch.bfloat16) if t.is_floating_point() else t)(torch.as_tensor(a)))
    def _A_setup():
        B, S, D = 1, 2048, 640
        P = D
        Lambda_re = -0.5 * (lambda _x: torch.exp(torch.as_tensor(_x)))((lambda *s: torch.randn(*s, dtype=torch.bfloat16) * 0.8)(P))
        Lambda_im = (lambda *s: torch.randn(*s, dtype=torch.bfloat16) * 0.8)(P) * 1.0
        B_re = (lambda *s: torch.randn(*s, dtype=torch.bfloat16) * 0.8)(P, D) * (D ** -0.5)
        B_im = (lambda *s: torch.randn(*s, dtype=torch.bfloat16) * 0.8)(P, D) * (D ** -0.5)
        C_re = (lambda *s: torch.randn(*s, dtype=torch.bfloat16) * 0.8)(D, P) * (P ** -0.5)
        C_im = (lambda *s: torch.randn(*s, dtype=torch.bfloat16) * 0.8)(D, P) * (P ** -0.5)
        log_dt = (lambda *s: torch.randn(*s, dtype=torch.bfloat16) * 0.8)(P) * 0.3 - 2.0
        Dskip = (lambda *s: torch.randn(*s, dtype=torch.bfloat16) * 0.8)(D)
        return {
            "x": _w((lambda *s: torch.randn(*s, dtype=torch.bfloat16) * 0.05)(B, S, D)),
            "Lambda_re": _w(Lambda_re),
            "Lambda_im": _w(Lambda_im),
            "B_re": _w(B_re),
            "B_im": _w(B_im),
            "C_re": _w(C_re),
            "C_im": _w(C_im),
            "log_dt": _w(log_dt),
            "Dskip": _w(Dskip),
        }
    _A_in = _A_setup()

    def _B_setup():
        B, S, D = 1, 2048, 640
        return {
            "x": _w((lambda *s: torch.randn(*s, dtype=torch.bfloat16) * 0.2)(B, S, D)),
            "wqkv": _w((lambda *s: torch.randn(*s, dtype=torch.bfloat16) * 0.2)(D, 3 * D)),
            "wo": _w((lambda *s: torch.randn(*s, dtype=torch.bfloat16) * 0.05)(D, D)),
        }
    _B_in = _B_setup()

    return {
        "A_x": _A_in["x"],
        "A_Lambda_re": _A_in["Lambda_re"],
        "A_Lambda_im": _A_in["Lambda_im"],
        "A_B_re": _A_in["B_re"],
        "A_B_im": _A_in["B_im"],
        "A_C_re": _A_in["C_re"],
        "A_C_im": _A_in["C_im"],
        "A_log_dt": _A_in["log_dt"],
        "A_Dskip": _A_in["Dskip"],
        "B_x": _B_in["x"],
        "B_wqkv": _B_in["wqkv"],
        "B_wo": _B_in["wo"],
    }

def reference(A_x, A_Lambda_re, A_Lambda_im, A_B_re, A_B_im, A_C_re, A_C_im, A_log_dt, A_Dskip, B_x, B_wqkv, B_wo):
    def _A_ref(x, Lambda_re, Lambda_im, B_re, B_im, C_re, C_im, log_dt, Dskip):
        Bsz, L, H = x.shape
        Lam = Lambda_re + 1j * Lambda_im
        Bmat = B_re + 1j * B_im
        Cmat = C_re + 1j * C_im
        dt = torch.exp(log_dt)
        Lam_bar = torch.exp(dt * Lam)
        B_bar = ((Lam_bar - 1.0) / Lam)[:, None] * Bmat
        Bu = torch.einsum("bld,pd->blp", x + 0j, B_bar)
        h = torch.zeros_like(Bu[:, 0, :])
        hs = []
        for t in range(L):
            h = Lam_bar[None, :] * h + Bu[:, t, :]
            hs.append(h)
        hseq = torch.stack(hs, 1)
        y = 2.0 * torch.einsum("blp,dp->bld", hseq, Cmat).real + x * Dskip[None, None, :]
        return y

    def _B_ref(x, wqkv, wo):
        B, S, D = x.shape
        H = 32; Dh = D // H
        qkv = (lambda x, *d: x.permute(*d))((x @ wqkv).reshape(B, S, 3, H, Dh), 2, 0, 3, 1, 4)
        q, k, v = qkv[0], qkv[1], qkv[2]
        scale = Dh ** -0.5
        scores = torch.einsum("bhqd,bhkd->bhqk", q, k) * scale
        p = _softmax(scores, axis=-1)
        o = torch.einsum("bhqk,bhkd->bhqd", p, v)
        return (lambda x, *d: x.permute(*d))(o, 0, 2, 1, 3).reshape(B, S, D) @ wo

    a = _A_ref(A_x, A_Lambda_re, A_Lambda_im, A_B_re, A_B_im, A_C_re, A_C_im, A_log_dt, A_Dskip)
    b = _B_ref(B_x, B_wqkv, B_wo)
    return torch.concatenate([a, b], axis=-1)
```

```cpp
#include <hip/hip_runtime.h>
#include <hip/hip_bf16.h>
#include <stdint.h>

typedef float f32x16 __attribute__((ext_vector_type(16)));
typedef float f32x8 __attribute__((ext_vector_type(8)));
typedef float f32x4 __attribute__((ext_vector_type(4)));
typedef float f32x2 __attribute__((ext_vector_type(2)));
typedef __bf16 bf16x8 __attribute__((ext_vector_type(8)));
typedef __bf16 bf16x16 __attribute__((ext_vector_type(16)));
typedef __bf16 bf16x2 __attribute__((ext_vector_type(2)));
typedef short s16x4 __attribute__((ext_vector_type(4)));
typedef short s16x8 __attribute__((ext_vector_type(8)));
typedef unsigned u32x4 __attribute__((ext_vector_type(4)));
typedef unsigned u32x2 __attribute__((ext_vector_type(2)));
typedef unsigned short u16;
typedef unsigned long long u64;
#define LDS_AS __attribute__((address_space(3)))
#define GLB_AS __attribute__((address_space(1)))

constexpr int SEQ = 2048, DM = 640, NH = 32, DH = 20;
constexpr int TILE_B = 1280;
constexpr int HEAD_B = 64 * TILE_B;
constexpr size_t WS_Q = 0;
constexpr size_t WS_K = WS_Q + (size_t)NH * HEAD_B;
constexpr size_t WS_V = WS_K + (size_t)NH * HEAD_B;
constexpr size_t WS_O = WS_V + (size_t)NH * HEAD_B;
constexpr size_t WS_H = WS_O + (size_t)SEQ * DM * 2;
constexpr size_t WS_E = WS_H + (size_t)2 * DM * SEQ * 2;
constexpr size_t WS_E_BYTES = (size_t)16 * DM * 2 * 8;

__device__ __forceinline__ float bf2f(u16 b) { return __uint_as_float((unsigned)b << 16); }
__device__ __forceinline__ unsigned pk2bf(float lo, float hi) {
    bf16x2 v = {(__bf16)lo, (__bf16)hi};
    return __builtin_bit_cast(unsigned, v);
}
__device__ __forceinline__ float rbf(float x) { return (float)(__bf16)x; }

__device__ __forceinline__ void glds16(const void* sbase, unsigned voff, unsigned lds_base) {
    unsigned sv;
    asm volatile("s_mov_b32 %0, m0\n\ts_mov_b32 m0, %3\n\ts_nop 0\n\tglobal_load_lds_dwordx4 %1, %2\n\ts_mov_b32 m0, %0"
                 : "=&s"(sv) : "v"(voff), "s"(sbase), "s"(lds_base) : "memory");
}
template <int N> __device__ __forceinline__ void wait_vm() { asm volatile("s_waitcnt vmcnt(%0)" :: "n"(N) : "memory"); }
__device__ __forceinline__ void raw_barrier() { asm volatile("s_waitcnt lgkmcnt(0)\n\ts_barrier" ::: "memory"); }

#define MFMA32(a, b, c) __builtin_amdgcn_mfma_f32_32x32x16_bf16(a, b, c, 0, 0, 0)

template <int N, bool KS> struct Operand {
    static constexpr int BYTES = N * 128;
    static constexpr int PIECES = N / 8;
    static constexpr int PPW = PIECES / 8;
    __device__ static __forceinline__ unsigned src_off(int p, int lane, int ld) {
        if constexpr (!KS) {
            const int row = 8 * p + (lane >> 3), c = (lane & 7) ^ ((row >> 1) & 7);
            return (unsigned)(row * ld * 2 + c * 16);
        } else if constexpr (N == 128) {
            const int kr = 4 * p + (lane >> 4), c = (lane & 15) ^ ((kr & 3) << 2);
            return (unsigned)(kr * ld * 2 + c * 16);
        } else {
            const int kr = 8 * p + (lane >> 3), c = (lane & 7) ^ (((kr >> 1) & 1) << 2);
            return (unsigned)(kr * ld * 2 + c * 16);
        }
    }
    __device__ static __forceinline__ unsigned frag_base_kc(int row, int h) { return (unsigned)(row * 128 + ((h ^ ((row >> 1) & 7)) << 4)); }
    __device__ static __forceinline__ unsigned frag_base_ks(int idx0, int lane) {
        const int g = lane >> 4, q = (lane >> 2) & 3, p = lane & 3;
        const int c = (idx0 >> 3) + 2 * (g & 1) + (p >> 1);
        if constexpr (N == 128) return (unsigned)((8 * (g >> 1) + q) * 256 + ((c ^ (q << 2)) << 4) + (p & 1) * 8);
        else return (unsigned)((8 * (g >> 1) + q) * 128 + ((c ^ (((q >> 1) & 1) << 2)) << 4) + (p & 1) * 8);
    }
    __device__ static __forceinline__ bf16x8 load(const LDS_AS char* img, unsigned base, int s) {
        if constexpr (!KS) {
            return *(const LDS_AS bf16x8*)(img + (base ^ (unsigned)(s << 5)));
        } else {
            typedef short v4i16_t __attribute__((ext_vector_type(4)));
            const LDS_AS char* a = img + base + s * 16 * (2 * N);
            v4i16_t lo = __builtin_amdgcn_ds_read_tr16_b64_v4i16((LDS_AS v4i16_t*)a);
            v4i16_t hi = __builtin_amdgcn_ds_read_tr16_b64_v4i16((LDS_AS v4i16_t*)(a + 4 * (2 * N)));
            s16x8 r = {lo[0], lo[1], lo[2], lo[3], hi[0], hi[1], hi[2], hi[3]};
            return __builtin_bit_cast(bf16x8, r);
        }
    }
};

template <int BP, bool PKS, int BQ, bool QKS, int MT, int NT, int NKT, int NS, int KSPL, bool SWAP, class FP, class FQ>
__device__ __forceinline__ void gemm_core(LDS_AS char* lds, f32x16 (&acc)[MT][NT], FP PB, int ldp, FQ QB, int ldq,
                                          const unsigned (&pfb)[MT], const unsigned (&qfb)[NT], int kgrp, int wid, int lane) {
    typedef Operand<BP, PKS> OP; typedef Operand<BQ, QKS> OQ;
    constexpr int STAGE = OP::BYTES + OQ::BYTES, LPW = OP::PPW + OQ::PPW;
    const unsigned lds0 = (unsigned)(uintptr_t)lds;
    unsigned poff[OP::PPW], qoff[OQ::PPW];
#pragma unroll
    for (int i = 0; i < OP::PPW; ++i) poff[i] = OP::src_off(wid * OP::PPW + i, lane, ldp);
#pragma unroll
    for (int i = 0; i < OQ::PPW; ++i) qoff[i] = OQ::src_off(wid * OQ::PPW + i, lane, ldq);
    auto issue = [&](int kt) {
        const unsigned b = lds0 + (kt % NS) * STAGE;
        const char* pb = PB(kt); const char* qb = QB(kt);
#pragma unroll
        for (int i = 0; i < OP::PPW; ++i) glds16(pb, poff[i], b + (wid * OP::PPW + i) * 1024);
#pragma unroll
        for (int i = 0; i < OQ::PPW; ++i) glds16(qb, qoff[i], b + OP::BYTES + (wid * OQ::PPW + i) * 1024);
    };
#pragma unroll
    for (int kt = 0; kt < NS - 1; ++kt) issue(kt);
#pragma unroll
    for (int kt = 0; kt < NKT; ++kt) {
        constexpr int dummy = 0; (void)dummy;
        const int rem = (kt + NS - 2 < NKT - 1 ? kt + NS - 2 : NKT - 1) - kt;
        if (rem >= 3) wait_vm<3 * LPW>(); else if (rem == 2) wait_vm<2 * LPW>(); else if (rem == 1) wait_vm<LPW>(); else wait_vm<0>();
        raw_barrier();
        if (kt + NS - 1 < NKT) issue(kt + NS - 1);
        const LDS_AS char* pi = lds + (kt % NS) * STAGE; const LDS_AS char* qi = pi + OP::BYTES;
#pragma unroll
        for (int ss = 0; ss < 4 / KSPL; ++ss) {
            const int s = (KSPL == 1) ? ss : (kgrp * (4 / KSPL) + ss);
            bf16x8 pf[MT], qf[NT];
#pragma unroll
            for (int m = 0; m < MT; ++m) pf[m] = OP::load(pi, pfb[m], s);
#pragma unroll
            for (int n = 0; n < NT; ++n) qf[n] = OQ::load(qi, qfb[n], s);
#pragma unroll
            for (int m = 0; m < MT; ++m)
#pragma unroll
                for (int n = 0; n < NT; ++n) acc[m][n] = SWAP ? MFMA32(qf[n], pf[m], acc[m][n]) : MFMA32(pf[m], qf[n], acc[m][n]);
        }
    }
}

constexpr float QSCALE = 0.22360679774997896f * 1.4426950408889634f;

template <bool ISV>
__device__ __forceinline__ void qkv_tile(LDS_AS char* lds, const u16* x, const u16* w, char* ws, int rt, int ct, int wid, int lane) {
    const int l31 = lane & 31, hb = lane >> 5;
    const int wm = wid & 1, wn = wid >> 1;
    f32x16 acc[2][1] = {};
    unsigned pfb[2], qfb[1];
    typedef Operand<128, false> OP; typedef Operand<128, true> OQ;
#pragma unroll
    for (int m = 0; m < 2; ++m) pfb[m] = OP::frag_base_kc(wm * 64 + m * 32 + l31, hb);
    qfb[0] = OQ::frag_base_ks(wn * 32, lane);
    const char* xb = (const char*)(x + (size_t)rt * 128 * DM);
    const char* wb = (const char*)(w + ct * 128);
    gemm_core<128, false, 128, true, 2, 1, 10, 4, 1, !ISV>(lds, acc,
        [&](int kt) { return xb + kt * 128; }, DM, [&](int kt) { return wb + (size_t)kt * 64 * (3 * DM) * 2; }, 3 * DM, pfb, qfb, 0, wid, lane);
    const int sec = ct / 5;
    const int fbase = (ct % 5) * 128 + wn * 32;
    char* dst = ws + (sec == 0 ? WS_Q : sec == 1 ? WS_K : WS_V);
    const float sc = (sec == 0) ? QSCALE : 1.0f;
#pragma unroll
    for (int m = 0; m < 2; ++m) {
        const int T = rt * 4 + wm * 2 + m;
#pragma unroll
        for (int g = 0; g < 4; ++g) {
            u32x2 v; v[0] = pk2bf(acc[m][0][4 * g] * sc, acc[m][0][4 * g + 1] * sc); v[1] = pk2bf(acc[m][0][4 * g + 2] * sc, acc[m][0][4 * g + 3] * sc);
            unsigned off;
            if constexpr (!ISV) {
                const int g4 = (fbase + 8 * g + 4 * hb) >> 2, head = g4 / 5, gg = g4 - head * 5;
                off = head * HEAD_B + T * TILE_B + (gg < 4 ? ((gg >> 1) * 32 + l31) * 16 + (gg & 1) * 8 : 1024 + l31 * 8);
            } else {
                const int f = fbase + l31, head = f / DH, d = f - head * DH;
                off = head * HEAD_B + T * TILE_B + (g >> 1) * 640 + ((g & 1) * 20 + d) * 16 + hb * 8;
            }
            *(u32x2*)(dst + off) = v;
        }
    }
}

__global__ void __launch_bounds__(512, 2) k_qkv(const u16* __restrict__ x, const u16* __restrict__ w, char* __restrict__ ws) {
    extern __shared__ __attribute__((aligned(16))) char smem[];
    LDS_AS char* lds = (LDS_AS char*)smem;
    const int tid = threadIdx.x, lane = tid & 63, wid = __builtin_amdgcn_readfirstlane(tid >> 6);
    { const int i = blockIdx.x * 512 + tid; if (i < (int)(WS_E_BYTES / 16)) *(u32x4*)(ws + WS_E + (size_t)i * 16) = u32x4{0u, 0u, 0u, 0u}; }
    const int xc = blockIdx.x & 7, j = blockIdx.x >> 3;
    const int half = xc >> 2, rg = xc & 3;
    const int lin = half * 30 + j;
    const int ct = lin >> 2, rt = rg * 4 + (lin & 3);
    if (ct < 10) qkv_tile<false>(lds, x, w, ws, rt, ct, wid, lane); else qkv_tile<true>(lds, x, w, ws, rt, ct, wid, lane);
}

struct cf { float re, im; };
__device__ __forceinline__ cf cmul(cf a, cf b) { return {a.re * b.re - a.im * b.im, a.re * b.im + a.im * b.re}; }
__device__ __forceinline__ cf cfma(cf a, cf b, cf c) { return {__builtin_fmaf(a.re, b.re, __builtin_fmaf(-a.im, b.im, c.re)), __builtin_fmaf(a.re, b.im, __builtin_fmaf(a.im, b.re, c.im))}; }

__device__ __forceinline__ void s1_tile(LDS_AS char* lds, const u16* x, const u16* bre, const u16* bim, const u16* lre, const u16* lim, const u16* ldt,
                                        char* ws, int c, int cg, int wid, int lane) {
    const int l31 = lane & 31, hb = lane >> 5;
    const int tq = wid & 3, g = wid >> 2;
    f32x16 acc[1][2] = {};
    typedef Operand<128, false> OP;
    const int rho = 16 * ((l31 >> 2) & 1) + (l31 & 3) + 4 * (l31 >> 3);
    unsigned pfb[1] = {OP::frag_base_kc(tq * 32 + rho, hb)};
    unsigned qfb[2] = {OP::frag_base_kc(g * 32 + l31, hb), OP::frag_base_kc(64 + g * 32 + l31, hb)};
    const char* xb = (const char*)(x + (size_t)c * 128 * DM);
    const char* qb = (const char*)((wid < 4 ? bre : bim) + (size_t)cg * 64 * DM) - (wid < 4 ? 0 : (size_t)64 * DM * 2);
    gemm_core<128, false, 128, false, 1, 2, 10, 4, 1, false>(lds, acc,
        [&](int kt) { return xb + kt * 128; }, DM, [&](int kt) { return qb + kt * 128; }, DM, pfb, qfb, 0, wid, lane);
    const int p = cg * 64 + g * 32 + l31;
    const float dt = rbf(expf(bf2f(ldt[p])));
    const cf lam = {bf2f(lre[p]), bf2f(lim[p])};
    cf lb; { const float e = expf(dt * lam.re); float sn, cs; sincosf(dt * lam.im, &sn, &cs); lb = {e * cs, e * sn}; }
    cf coef; { const float nr = lb.re - 1.0f, ni = lb.im, den = lam.re * lam.re + lam.im * lam.im;
               coef = {(nr * lam.re + ni * lam.im) / den, (ni * lam.re - nr * lam.im) / den}; }
    cf l16 = lb;
#pragma unroll
    for (int i = 0; i < 4; ++i) l16 = cmul(l16, l16);
    cf E = {0.f, 0.f};
#pragma unroll
    for (int r = 0; r < 16; ++r) E = cfma(lb, E, cf{acc[0][0][r], acc[0][1][r]});
    raw_barrier();
    LDS_AS f32x2* segE = (LDS_AS f32x2*)lds;
    LDS_AS f32x2* segC = segE + 8 * 64;
    const int seg = tq * 2 + hb, chl = g * 32 + l31;
    segE[seg * 64 + chl] = f32x2{E.re, E.im};
    raw_barrier();
    if (wid == 0) {
        const int pc = cg * 64 + lane;
        const float dt2 = rbf(expf(bf2f(ldt[pc])));
        const cf lam2 = {bf2f(lre[pc]), bf2f(lim[pc])};
        cf lb2; { const float e = expf(dt2 * lam2.re); float sn, cs; sincosf(dt2 * lam2.im, &sn, &cs); lb2 = {e * cs, e * sn}; }
        cf m16 = lb2;
#pragma unroll
        for (int i = 0; i < 4; ++i) m16 = cmul(m16, m16);
        cf m128 = m16;
#pragma unroll
        for (int i = 0; i < 3; ++i) m128 = cmul(m128, m128);
        cf se[8];
#pragma unroll
        for (int s = 0; s < 8; ++s) { f32x2 v = segE[s * 64 + lane]; se[s] = {v[0], v[1]}; }
        cf B = {0.f, 0.f};
#pragma unroll
        for (int s = 0; s < 8; ++s) B = cfma(m16, B, se[s]);
        GLB_AS u64* gr = (GLB_AS u64*)(ws + WS_E);
        if (c < 15) {
            __hip_atomic_store(gr + ((size_t)c * DM + pc) * 2, ((u64)1 << 32) | __float_as_uint(B.re), __ATOMIC_RELAXED, __HIP_MEMORY_SCOPE_AGENT);
            __hip_atomic_store(gr + ((size_t)c * DM + pc) * 2 + 1, ((u64)1 << 32) | __float_as_uint(B.im), __ATOMIC_RELAXED, __HIP_MEMORY_SCOPE_AGENT);
        }
        cf H = {0.f, 0.f};
        for (int cp = 0; cp < c; ++cp) {
            u64 a, b;
            unsigned spins = 0;
            while (true) {
                a = __hip_atomic_load(gr + ((size_t)cp * DM + pc) * 2, __ATOMIC_RELAXED, __HIP_MEMORY_SCOPE_AGENT);
                b = __hip_atomic_load(gr + ((size_t)cp * DM + pc) * 2 + 1, __ATOMIC_RELAXED, __HIP_MEMORY_SCOPE_AGENT);
                if (__all((a >> 32) == 1 && (b >> 32) == 1)) break;
                if (++spins > (1u << 22)) break;
                __builtin_amdgcn_s_sleep(1);
            }
            H = cfma(m128, H, cf{__uint_as_float((unsigned)a), __uint_as_float((unsigned)b)});
        }
#pragma unroll
        for (int s = 0; s < 8; ++s) { segC[s * 64 + lane] = f32x2{H.re, H.im}; H = cfma(m16, H, se[s]); }
    }
    raw_barrier();
    f32x2 c0 = segC[seg * 64 + chl];
    cf h = {c0[0], c0[1]};
    const cf c2 = {2.0f * coef.re, 2.0f * coef.im};
    unsigned ore[8], oim[8];
#pragma unroll
    for (int r = 0; r < 16; r += 2) {
        h = cfma(lb, h, cf{acc[0][0][r], acc[0][1][r]});     const cf a = cmul(c2, h);
        h = cfma(lb, h, cf{acc[0][0][r + 1], acc[0][1][r + 1]}); const cf b = cmul(c2, h);
        ore[r >> 1] = pk2bf(a.re, b.re); oim[r >> 1] = pk2bf(-a.im, -b.im);
    }
    u16* hT = (u16*)(ws + WS_H);
    const int t0 = c * 128 + tq * 32 + hb * 16;
    u32x4* dre = (u32x4*)(hT + (size_t)p * SEQ + t0); u32x4* dim = (u32x4*)(hT + (size_t)(DM + p) * SEQ + t0);
    dre[0] = u32x4{ore[0], ore[1], ore[2], ore[3]}; dre[1] = u32x4{ore[4], ore[5], ore[6], ore[7]};
    dim[0] = u32x4{oim[0], oim[1], oim[2], oim[3]}; dim[1] = u32x4{oim[4], oim[5], oim[6], oim[7]};
}

__global__ void __launch_bounds__(512, 2) k_s1(const u16* __restrict__ x, const u16* __restrict__ bre, const u16* __restrict__ bim,
                                               const u16* __restrict__ lre, const u16* __restrict__ lim, const u16* __restrict__ ldt, char* __restrict__ ws) {
    extern __shared__ __attribute__((aligned(16))) char smem[];
    const int tid = threadIdx.x, lane = tid & 63, wid = __builtin_amdgcn_readfirstlane(tid >> 6);
    const int b = blockIdx.x;
    s1_tile((LDS_AS char*)smem, x, bre, bim, lre, lim, ldt, ws, b / 10, b % 10, wid, lane);
}

#define STR2(x) #x
#define XSTR(x) STR2(x)
#define E1(r) "v_exp_f32 v" XSTR(r) ", v" XSTR(r) "\n\t"
#define ASTEP(C0, C7, C8, C15, N0, N8, N15, T, e0, e1, e2, e3, e4, e5, e6, e7, e8, e9, e10, e11, e12, e13, e14, e15) \
    "s_waitcnt lgkmcnt(3)\n\ts_nop 0\n\t" \
    "v_smfmac_f32_32x32x32_bf16 v[16:31], v[76:79], v[" XSTR(N8) ":" XSTR(N15) "], v86\n\t" \
    "s_mov_b64 exec, %[vm]\n\tds_read_b128 v[76:79], v89 offset:1280*" #T "+640\n\ts_mov_b64 exec, -1\n\t" \
    E1(e0) E1(e1) E1(e2) E1(e3) \
    "s_waitcnt lgkmcnt(3)\n\t" \
    "v_mfma_f32_32x32x16_bf16 v[" XSTR(N0) ":" XSTR(N15) "], v[64:67], v[80:83], 0\n\t" \
    E1(e4) E1(e5) E1(e6) E1(e7) \
    "s_waitcnt lgkmcnt(2)\n\t" \
    "v_mfma_f32_32x32x8_bf16 v[" XSTR(N0) ":" XSTR(N15) "], v[68:69], v[84:85], v[" XSTR(N0) ":" XSTR(N15) "]\n\t" \
    "ds_read_b128 v[64:67], v87 offset:1280*" #T "+2560\n\tds_read_b64 v[68:69], v88 offset:1280*" #T "+2560\n\t" \
    E1(e8) E1(e9) E1(e10) E1(e11) \
    "s_waitcnt lgkmcnt(3)\n\t" \
    "v_smfmac_f32_32x32x32_bf16 v[16:31], v[72:75], v[" XSTR(C0) ":" XSTR(C7) "], v86\n\t" \
    "s_mov_b64 exec, %[vm]\n\tds_read_b128 v[72:75], v89 offset:1280*" #T "+1280\n\ts_mov_b64 exec, -1\n\t" \
    E1(e12) E1(e13) E1(e14) E1(e15)
#define STEPA(T) ASTEP(32, 39, 40, 47, 48, 56, 63, T, 32, 33, 34, 35, 36, 37, 38, 39, 40, 41, 42, 43, 44, 45, 46, 47)
#define STEPB(T) ASTEP(48, 55, 56, 63, 32, 40, 47, T, 48, 49, 50, 51, 52, 53, 54, 55, 56, 57, 58, 59, 60, 61, 62, 63)

__device__ __forceinline__ void attn_fill(const char* Kg, const char* Vg, unsigned lds0, int wid, int lane) {
#pragma unroll
    for (int i = 0; i < 5; ++i) { const int p = i * 16 + wid; glds16(Kg + p * 1024, lane * 16, lds0 + p * 1024); glds16(Vg + p * 1024, lane * 16, lds0 + HEAD_B + p * 1024); }
}

__global__ void __launch_bounds__(1024) k_attn(char* __restrict__ ws) {
    extern __shared__ __attribute__((aligned(16))) char smem[];
    LDS_AS char* lds = (LDS_AS char*)smem;
    const int tid = threadIdx.x, lane = tid & 63, wid = __builtin_amdgcn_readfirstlane(tid >> 6);
    const int x = blockIdx.x & 7, j = blockIdx.x >> 3, head = 4 * x + (j >> 3), qt = j & 7;
    const unsigned lds0 = (unsigned)(uintptr_t)lds;
    const char* Kg = ws + WS_K + (size_t)head * HEAD_B; const char* Vg = ws + WS_V + (size_t)head * HEAD_B;
    attn_fill(Kg, Vg, lds0, wid, lane);
    const int wq = wid & 7, kh = wid >> 3, l31 = lane & 31, hb = lane >> 5;
    const char* qp = ws + WS_Q + (size_t)head * HEAD_B + (qt * 8 + wq) * TILE_B;
    const bf16x8 qf1 = *(const bf16x8*)(qp + lane * 16);
    u32x2 q2 = *(const u32x2*)(qp + 1024 + l31 * 8); if (hb) q2 = u32x2{0u, 0u};
    const s16x4 qf2 = __builtin_bit_cast(s16x4, q2);
    asm volatile("s_waitcnt vmcnt(0)" ::: "memory");
    __syncthreads();
    f32x16 O = {};
    const unsigned ones = (l31 == 20) ? 0x3F803F80u : 0u;
    {
        unsigned ka = lds0 + kh * 32 * TILE_B + lane * 16, k2a = lds0 + kh * 32 * TILE_B + 1024 + l31 * 8;
        unsigned va = lds0 + HEAD_B + kh * 32 * TILE_B + (hb * 20 + l31) * 16;
        u32x4 vf0 = {ones, ones, ones, ones}, vf1 = vf0;
        const int idx = 0xDDDDDDDD;
        const u64 vmask = 0x000FFFFF000FFFFFull;
        int cnt = 4;
        f32x16 zero16 = {};
        asm volatile(
            "ds_read_b128 v[64:67], v87\n\tds_read_b64 v[68:69], v88\n\t"
            "s_waitcnt lgkmcnt(0)\n\t"
            "v_mfma_f32_32x32x16_bf16 v[32:47], v[64:67], v[80:83], 0\n\t"
            "v_mfma_f32_32x32x8_bf16 v[32:47], v[68:69], v[84:85], v[32:47]\n\t"
            "s_nop 7\n\t"
            "s_mov_b64 exec, %[vm]\n\tds_read_b128 v[76:79], v89 offset:640\n\ts_mov_b64 exec, -1\n\t"
            "ds_read_b128 v[64:67], v87 offset:1280\n\tds_read_b64 v[68:69], v88 offset:1280\n\t"
            "s_mov_b64 exec, %[vm]\n\tds_read_b128 v[72:75], v89\n\ts_mov_b64 exec, -1\n\t"
            "s_nop 7\n\t"
            "1:\n\t"
            STEPA(0) STEPB(1) STEPA(2) STEPB(3) STEPA(4) STEPB(5) STEPA(6) STEPB(7)
            "v_add_u32 v87, 10240, v87\n\tv_add_u32 v88, 10240, v88\n\tv_add_u32 v89, 10240, v89\n\t"
            "s_sub_u32 %[cnt], %[cnt], 1\n\ts_cmp_lg_u32 %[cnt], 0\n\ts_cbranch_scc1 1b\n\t"
            "s_waitcnt lgkmcnt(0)\n\ts_nop 1\n\t"
            "v_smfmac_f32_32x32x32_bf16 v[16:31], v[76:79], v[56:63], v86\n\t"
            "s_nop 15\n\t"
            : "+{v[16:31]}"(O), "+{v[48:63]}"(zero16), "+{v[72:75]}"(vf0), "+{v[76:79]}"(vf1), "+{v87}"(ka), "+{v88}"(k2a), "+{v89}"(va), [cnt] "+s"(cnt)
            : "{v[80:83]}"(qf1), "{v[84:85]}"(qf2), "{v86}"(idx), [vm] "s"(vmask)
            : "v32", "v33", "v34", "v35", "v36", "v37", "v38", "v39", "v40", "v41", "v42", "v43", "v44", "v45", "v46", "v47",
              "v64", "v65", "v66", "v67", "v68", "v69", "scc", "memory");
    }
    __syncthreads();
    LDS_AS float* st = (LDS_AS float*)lds + wq * 64 * 12;
    volatile LDS_AS int* flag = (volatile LDS_AS int*)(lds + 32768);
    if (tid == 0) *flag = 0;
    if (kh == 1) {
#pragma unroll
        for (int r = 0; r < 12; ++r) st[r * 64 + lane] = O[r];
    }
    __syncthreads();
    int bad = 0;
    if (kh == 0) {
#pragma unroll
        for (int r = 0; r < 12; ++r) O[r] += st[r * 64 + lane];
        const float l = __shfl(O[8], l31 + 32);
        float amax = 0.f;
#pragma unroll
        for (int r = 0; r < 12; ++r) amax = fmaxf(amax, fabsf(O[r]));
        bad = !(l >= 5.421010862427522e-20f && l <= 1.8446744073709552e19f && amax < 3.0e38f);
    }
    if (bad) *flag = 1;
    __syncthreads();
    if (*flag) {
        __syncthreads();
        attn_fill(Kg, Vg, lds0, wid, lane);
        asm volatile("s_waitcnt vmcnt(0)" ::: "memory");
        __syncthreads();
        if (kh == 0) {
            f32x16 A = {}; float m = -INFINITY;
            u32x4 vf0 = {ones, ones, ones, ones}, vf1 = vf0;
            for (int t = 0; t < 64; ++t) {
                const LDS_AS char* kt = lds + t * TILE_B;
                const bf16x8 kf1 = *(const LDS_AS bf16x8*)(kt + lane * 16);
                const s16x4 kf2 = *(const LDS_AS s16x4*)(kt + 1024 + l31 * 8);
                if (l31 < 20) { vf0 = *(const LDS_AS u32x4*)(kt + HEAD_B + (hb * 20 + l31) * 16); vf1 = *(const LDS_AS u32x4*)(kt + HEAD_B + 640 + (hb * 20 + l31) * 16); }
                f32x16 s = MFMA32(kf1, qf1, f32x16{});
                s = __builtin_amdgcn_mfma_f32_32x32x8bf16_1k(kf2, qf2, s, 0, 0, 0);
                float tm = s[0];
#pragma unroll
                for (int r = 1; r < 16; ++r) tm = fmaxf(tm, s[r]);
                tm = fmaxf(tm, __shfl_xor(tm, 32));
                const float mn = fmaxf(m, tm);
                const float al = __builtin_amdgcn_exp2f(m - mn);
                m = mn;
#pragma unroll
                for (int r = 0; r < 16; ++r) { A[r] *= al; s[r] = __builtin_amdgcn_exp2f(s[r] - mn); }
                A = __builtin_amdgcn_smfmac_f32_32x32x32_bf16(__builtin_bit_cast(bf16x8, vf0), __builtin_bit_cast(bf16x16, __builtin_shufflevector(s, s, 0, 1, 2, 3, 4, 5, 6, 7)), A, 0xDDDDDDDD, 0, 0);
                A = __builtin_amdgcn_smfmac_f32_32x32x32_bf16(__builtin_bit_cast(bf16x8, vf1), __builtin_bit_cast(bf16x16, __builtin_shufflevector(s, s, 8, 9, 10, 11, 12, 13, 14, 15)), A, 0xDDDDDDDD, 0, 0);
            }
            O = A;
        }
    }
    if (kh == 0) {
        const float rl = 1.0f / __shfl(O[8], l31 + 32);
        u16* op = (u16*)(ws + WS_O) + (size_t)(qt * 256 + wq * 32 + l31) * DM + head * DH;
#pragma unroll
        for (int g = 0; g < 3; ++g) {
            const int d0 = 8 * g + 4 * hb;
            if (d0 < 20) { u32x2 v; v[0] = pk2bf(O[4 * g] * rl, O[4 * g + 1] * rl); v[1] = pk2bf(O[4 * g + 2] * rl, O[4 * g + 3] * rl); *(u32x2*)(op + d0) = v; }
        }
    }
}

template <class ACC>
__device__ __forceinline__ void ksplit_reduce(LDS_AS char* lds, ACC& a0, ACC& a1, int kgrp, int slot, int lane) {
    raw_barrier();
    LDS_AS float* st = (LDS_AS float*)lds + slot * 64 * 32;
    if (kgrp == 1) {
#pragma unroll
        for (int r = 0; r < 16; ++r) { st[r * 64 + lane] = a0[r]; st[(16 + r) * 64 + lane] = a1[r]; }
    }
    raw_barrier();
    if (kgrp == 0) {
#pragma unroll
        for (int r = 0; r < 16; ++r) { a0[r] += st[r * 64 + lane]; a1[r] += st[(16 + r) * 64 + lane]; }
    }
}

__device__ __forceinline__ void s2_tile(LDS_AS char* lds, const u16* x, const u16* cre, const u16* cim, const u16* dsk, const char* ws, float* out, int rt, int ct, int wid, int lane) {
    const int l31 = lane & 31, hb = lane >> 5;
    const int kgrp = wid >> 2, wm = wid & 1, wn = (wid >> 1) & 1;
    f32x16 acc[2][1] = {};
    typedef Operand<128, true> OP; typedef Operand<64, false> OQ;
    unsigned pfb[2] = {OP::frag_base_ks(wm * 64, lane), OP::frag_base_ks(wm * 64 + 32, lane)};
    unsigned qfb[1] = {OQ::frag_base_kc(wn * 32 + l31, hb)};
    const char* hb_ = ws + WS_H + (size_t)rt * 128 * 2;
    const char* cr = (const char*)(cre + (size_t)ct * 64 * DM); const char* ci = (const char*)(cim + (size_t)ct * 64 * DM);
    gemm_core<128, true, 64, false, 2, 1, 20, 3, 2, false>(lds, acc,
        [&](int kt) { return hb_ + (size_t)kt * 64 * SEQ * 2; }, SEQ, [&](int kt) { return kt < 10 ? cr + kt * 128 : ci + (kt - 10) * 128; }, DM, pfb, qfb, kgrp, wid, lane);
    ksplit_reduce(lds, acc[0][0], acc[1][0], kgrp, wid & 3, lane);
    if (kgrp == 0) {
        const int d = ct * 64 + wn * 32 + l31;
        const float ds = bf2f(dsk[d]);
#pragma unroll
        for (int m = 0; m < 2; ++m)
#pragma unroll
            for (int r = 0; r < 16; ++r) {
                const int t = rt * 128 + wm * 64 + m * 32 + (r & 3) + 8 * (r >> 2) + 4 * hb;
                out[(size_t)t * (2 * DM) + d] = acc[m][0][r] + rbf(bf2f(x[(size_t)t * DM + d]) * ds);
            }
    }
}

__device__ __forceinline__ void oproj_tile(LDS_AS char* lds, const u16* wo, const char* ws, float* out, int rt, int ct, int wid, int lane) {
    const int l31 = lane & 31, hb = lane >> 5;
    const int kgrp = wid >> 2, wm = wid & 1, wn = (wid >> 1) & 1;
    f32x16 acc[2][1] = {};
    typedef Operand<128, false> OP; typedef Operand<64, true> OQ;
    unsigned pfb[2] = {OP::frag_base_kc(wm * 64 + l31, hb), OP::frag_base_kc(wm * 64 + 32 + l31, hb)};
    unsigned qfb[1] = {OQ::frag_base_ks(wn * 32, lane)};
    const char* ob = ws + WS_O + (size_t)rt * 128 * DM * 2;
    const char* wb = (const char*)(wo + ct * 64);
    gemm_core<128, false, 64, true, 2, 1, 10, 3, 2, false>(lds, acc,
        [&](int kt) { return ob + kt * 128; }, DM, [&](int kt) { return wb + (size_t)kt * 64 * DM * 2; }, DM, pfb, qfb, kgrp, wid, lane);
    ksplit_reduce(lds, acc[0][0], acc[1][0], kgrp, wid & 3, lane);
    if (kgrp == 0) {
        const int n = ct * 64 + wn * 32 + l31;
#pragma unroll
        for (int m = 0; m < 2; ++m)
#pragma unroll
            for (int r = 0; r < 16; ++r) {
                const int t = rt * 128 + wm * 64 + m * 32 + (r & 3) + 8 * (r >> 2) + 4 * hb;
                out[(size_t)t * (2 * DM) + DM + n] = rbf(acc[m][0][r]);
            }
    }
}

__global__ void __launch_bounds__(512, 4) k_out(const u16* __restrict__ ax, const u16* __restrict__ cre, const u16* __restrict__ cim, const u16* __restrict__ dsk,
                                                const u16* __restrict__ wo, const char* __restrict__ ws, float* __restrict__ out) {
    extern __shared__ __attribute__((aligned(16))) char smem[];
    const int tid = threadIdx.x, lane = tid & 63, wid = __builtin_amdgcn_readfirstlane(tid >> 6);
    const int b = blockIdx.x;
    if (b < 160) s2_tile((LDS_AS char*)smem, ax, cre, cim, dsk, ws, out, b / 10, b % 10, wid, lane);
    else oproj_tile((LDS_AS char*)smem, wo, ws, out, (b - 160) / 10, (b - 160) % 10, wid, lane);
}

extern "C" void kernel_launch(void* const* d_in, const int* in_sizes, int n_in, void* d_out, int out_size, void* d_ws, size_t ws_size, hipStream_t stream) {
    const u16* A_x = (const u16*)d_in[0];
    const u16* L_re = (const u16*)d_in[1];
    const u16* L_im = (const u16*)d_in[2];
    const u16* B_re = (const u16*)d_in[3];
    const u16* B_im = (const u16*)d_in[4];
    const u16* C_re = (const u16*)d_in[5];
    const u16* C_im = (const u16*)d_in[6];
    const u16* logdt = (const u16*)d_in[7];
    const u16* Dsk = (const u16*)d_in[8];
    const u16* B_x = (const u16*)d_in[9];
    const u16* wqkv = (const u16*)d_in[10];
    const u16* wo = (const u16*)d_in[11];
    char* ws = (char*)d_ws;
    float* out = (float*)d_out;
    (void)hipFuncSetAttribute((const void*)k_qkv, hipFuncAttributeMaxDynamicSharedMemorySize, 4 * 32768);
    (void)hipFuncSetAttribute((const void*)k_s1, hipFuncAttributeMaxDynamicSharedMemorySize, 4 * 32768);
    (void)hipFuncSetAttribute((const void*)k_attn, hipFuncAttributeMaxDynamicSharedMemorySize, 163840);
    (void)hipFuncSetAttribute((const void*)k_out, hipFuncAttributeMaxDynamicSharedMemorySize, 3 * 24576);
    k_qkv<<<240, 512, 4 * 32768, stream>>>(B_x, wqkv, ws);
    k_s1<<<160, 512, 4 * 32768, stream>>>(A_x, B_re, B_im, L_re, L_im, logdt, ws);
    k_attn<<<256, 1024, 163840, stream>>>(ws);
    k_out<<<320, 512, 3 * 24576, stream>>>(A_x, C_re, C_im, Dsk, wo, ws, out);
}
```

```cpp
#include <hip/hip_runtime.h>
#include <hip/hip_bf16.h>
#include <stdint.h>

#if defined(__HIP_DEVICE_COMPILE__) && __HIP_DEVICE_COMPILE__
#pragma clang attribute push(__attribute__((target("no-packed-fp32-ops"))), apply_to = function)
#endif

typedef float f32x16 __attribute__((ext_vector_type(16)));
typedef float f32x8 __attribute__((ext_vector_type(8)));
typedef float f32x4 __attribute__((ext_vector_type(4)));
typedef float f32x2 __attribute__((ext_vector_type(2)));
typedef __bf16 bf16x8 __attribute__((ext_vector_type(8)));
typedef __bf16 bf16x16 __attribute__((ext_vector_type(16)));
typedef __bf16 bf16x2 __attribute__((ext_vector_type(2)));
typedef short s16x4 __attribute__((ext_vector_type(4)));
typedef short s16x8 __attribute__((ext_vector_type(8)));
typedef unsigned u32x4 __attribute__((ext_vector_type(4)));
typedef unsigned u32x2 __attribute__((ext_vector_type(2)));
typedef unsigned short u16;
typedef unsigned long long u64;
#define LDS_AS __attribute__((address_space(3)))
#define GLB_AS __attribute__((address_space(1)))

constexpr int SEQ = 2048, DM = 640, NH = 32, DH = 20;
constexpr int TILE_B = 1280;
constexpr int HEAD_B = 64 * TILE_B;
constexpr int CHUNK_B = 8 * TILE_B;
__host__ __device__ constexpr int kv_tile_off(int T) { return ((T & 31) >> 1) * CHUNK_B + ((T >> 5) * 2 + (T & 1)) * TILE_B; }
constexpr size_t WS_Q = 0;
constexpr size_t WS_KV = WS_Q + (size_t)NH * HEAD_B;
constexpr size_t WS_O = WS_KV + (size_t)NH * 2 * HEAD_B + 65536;
constexpr size_t WS_H = WS_O + (size_t)SEQ * DM * 2;
constexpr size_t WS_E = WS_H + (size_t)2 * DM * SEQ * 2;
constexpr size_t WS_EPOCH = WS_E + (size_t)16 * DM * 2 * 8;

__device__ __forceinline__ float u2f(unsigned u) { return __builtin_bit_cast(float, u); }
__device__ __forceinline__ unsigned f2u(float f) { return __builtin_bit_cast(unsigned, f); }
__device__ __forceinline__ bool wave_all(bool p) { return __builtin_amdgcn_ballot_w64(!p) == 0; }
__device__ __forceinline__ float lane_get(float v, int src) { return u2f((unsigned)__builtin_amdgcn_ds_bpermute(src << 2, (int)f2u(v))); }
__device__ __forceinline__ float bf2f(u16 b) { return u2f((unsigned)b << 16); }
__device__ __forceinline__ unsigned pk2bf(float lo, float hi) {
    bf16x2 v = {(__bf16)lo, (__bf16)hi};
    return __builtin_bit_cast(unsigned, v);
}
__device__ __forceinline__ float rbf(float x) { return (float)(__bf16)x; }

__device__ __forceinline__ void glds16(const void* sbase, unsigned voff, unsigned lds_base) {
    unsigned sv;
    asm volatile("s_mov_b32 %0, m0\n\ts_mov_b32 m0, %3\n\ts_nop 0\n\tglobal_load_lds_dwordx4 %1, %2\n\ts_mov_b32 m0, %0"
                 : "=&s"(sv) : "v"(voff), "s"(sbase), "s"(lds_base) : "memory");
}
template <int N> __device__ __forceinline__ void wait_vm() { asm volatile("s_waitcnt vmcnt(%0)" :: "n"(N) : "memory"); }
__device__ __forceinline__ void raw_barrier() { asm volatile("s_waitcnt lgkmcnt(0)\n\ts_barrier" ::: "memory"); }

struct Stamps {};
#define STAMP_INIT(S) Stamps S
#define STAMP(S, slot) do {} while (0)
#define STAMP_FLUSH(S, ws, kid) do {} while (0)
__device__ __forceinline__ int fresh_lane() {
    int l; asm volatile("v_mbcnt_lo_u32_b32 %0, -1, 0\n\tv_mbcnt_hi_u32_b32 %0, -1, %0" : "=v"(l)); return l;
}

#define MFMA32(a, b, c) __builtin_amdgcn_mfma_f32_32x32x16_bf16(a, b, c, 0, 0, 0)

template <int N, bool KS, int BK> struct Operand {
    static constexpr int BYTES = N * BK * 2;
    static constexpr int PIECES = BYTES / 1024;
    __device__ static __forceinline__ int kc_swz(int row) { return BK == 64 ? ((row >> 1) & 7) : ((row >> 2) & 3); }
    __device__ static __forceinline__ unsigned src_off(int p, int lane, int ld) {
        if constexpr (!KS) {
            constexpr int CPR = BK / 8;
            const int row = (64 / CPR) * p + lane / CPR, c = (lane % CPR) ^ kc_swz(row);
            return (unsigned)(row * ld * 2 + c * 16);
        } else if constexpr (N == 160) {
            const int q = 64 * p + lane, kr = q / 20, c = q - kr * 20;
            return (unsigned)(kr * ld * 2 + c * 16);
        } else if constexpr (N == 128) {
            const int kr = 4 * p + (lane >> 4), c = (lane & 15) ^ ((kr & 3) << 2);
            return (unsigned)(kr * ld * 2 + c * 16);
        } else {
            const int kr = 8 * p + (lane >> 3), c = (lane & 7) ^ (((kr >> 1) & 1) << 2);
            return (unsigned)(kr * ld * 2 + c * 16);
        }
    }
    __device__ static __forceinline__ unsigned frag_base_kc(int row, int h) { return (unsigned)(row * (2 * BK) + ((h ^ kc_swz(row)) << 4)); }
    __device__ static __forceinline__ unsigned frag_base_ks(int idx0, int lane) {
        const int g = lane >> 4, q = (lane >> 2) & 3, p = lane & 3;
        const int c = (idx0 >> 3) + 2 * (g & 1) + (p >> 1);
        if constexpr (N == 160) return (unsigned)((8 * (g >> 1) + q) * 320 + (c << 4) + (p & 1) * 8);
        else if constexpr (N == 128) return (unsigned)((8 * (g >> 1) + q) * 256 + ((c ^ (q << 2)) << 4) + (p & 1) * 8);
        else return (unsigned)((8 * (g >> 1) + q) * 128 + ((c ^ (((q >> 1) & 1) << 2)) << 4) + (p & 1) * 8);
    }
    __device__ static __forceinline__ bf16x8 load(unsigned img, unsigned base, int s) {
        if constexpr (!KS) {
            return *(const LDS_AS bf16x8*)(uintptr_t)((base ^ (unsigned)(s << 5)) + img);
        } else {
            typedef short v4i16_t __attribute__((ext_vector_type(4)));
            const LDS_AS char* a = (const LDS_AS char*)(uintptr_t)(base + img) + s * 16 * (2 * N);
            v4i16_t lo = __builtin_amdgcn_ds_read_tr16_b64_v4i16((LDS_AS v4i16_t*)a);
            v4i16_t hi = __builtin_amdgcn_ds_read_tr16_b64_v4i16((LDS_AS v4i16_t*)(a + 4 * (2 * N)));
            s16x8 r = {lo[0], lo[1], lo[2], lo[3], hi[0], hi[1], hi[2], hi[3]};
            return __builtin_bit_cast(bf16x8, r);
        }
    }
};

__device__ __forceinline__ void prefetch_line(unsigned& d, const void* p) { asm volatile("global_load_dword %0, %1, off" : "+v"(d) : "v"(p) : "memory"); }
__device__ __forceinline__ void prefetch_done(unsigned& d) { asm volatile("s_waitcnt vmcnt(0)" : "+v"(d) :: "memory"); }
template <int NKT> __device__ __forceinline__ int krot(int kt, int off) { const int k = kt + off; return k >= NKT ? k - NKT : k; }
__device__ __forceinline__ void bare_barrier() { asm volatile("s_barrier" ::: "memory"); }
template <int LPW, int R> __device__ __forceinline__ void wait_stages(int rem) {
    if constexpr (R == 0) wait_vm<0>();
    else { if (rem >= R) wait_vm<R * LPW>(); else wait_stages<LPW, R - 1>(rem); }
}

template <int BP, bool PKS, int BQ, bool QKS, int BK, int NKT, int NS, int D0, int QPX = 0, class FP, class FQ>
__device__ __forceinline__ void gemm_producer(LDS_AS char* lds, FP PB, int ldp, FQ QB, int ldq, int pw, int lane, Stamps& ST, int q0 = -1) {
    typedef Operand<BP, PKS, BK> OP; typedef Operand<BQ, QKS, BK> OQ;
    constexpr int STAGE = OP::BYTES + OQ::BYTES, PP = OP::PIECES / 4, QP = QPX ? QPX : OQ::PIECES / 4, LPW = PP + QP;
    if (QPX == 0) q0 = pw * QP;
    static_assert((NS - 2) * LPW <= 63 && D0 <= NS - 1 && D0 >= 2, "vmcnt range");
    const unsigned lds0 = (unsigned)(uintptr_t)lds;
    unsigned poff[PP], qoff[QP];
#pragma unroll
    for (int i = 0; i < PP; ++i) poff[i] = OP::src_off(pw * PP + i, lane, ldp);
#pragma unroll
    for (int i = 0; i < QP; ++i) qoff[i] = OQ::src_off(q0 + i, lane, ldq);
    auto issue = [&](int kt) {
        const unsigned b = lds0 + (kt % NS) * STAGE;
        const char* pb = PB(kt); const char* qb = QB(kt);
#pragma unroll
        for (int i = 0; i < PP; ++i) glds16(pb, poff[i], b + (pw * PP + i) * 1024);
#pragma unroll
        for (int i = 0; i < QP; ++i) glds16(qb, qoff[i], b + OP::BYTES + (q0 + i) * 1024);
    };
#pragma unroll
    for (int kt = 0; kt < D0; ++kt) issue(kt);
    wait_vm<(D0 - 1) * LPW>();
    bare_barrier();
    STAMP(ST, 2);
    int issued = D0;
#pragma unroll
    for (int kt = 0; kt < NKT - 1; ++kt) {
        wait_stages<LPW, NS - 2>(issued - (kt + 2));
        bare_barrier();
        const int cap = (kt + NS < NKT) ? kt + NS : NKT;
        if (issued < cap) { issue(issued); ++issued; }
        if (issued < cap) { issue(issued); ++issued; }
    }
}

template <int BP, bool PKS, int BQ, bool QKS, int BK, int MT, int NT, int NKT, int NS, bool SWAP, int PFP, int PFQ>
__device__ __forceinline__ void gemm_consumer(LDS_AS char* lds, f32x16 (&acc)[MT][NT], const unsigned (&pfb)[MT], const unsigned (&qfb)[NT], Stamps& ST) {
    typedef Operand<BP, PKS, BK> OP; typedef Operand<BQ, QKS, BK> OQ;
    constexpr int STAGE = OP::BYTES + OQ::BYTES, KST = BK / 16, NG = KST * NKT, PFM = PFP > PFQ ? PFP : PFQ;
    static_assert(PFM <= KST, "prefetch may reach at most one stage ahead");
    bare_barrier();
    STAMP(ST, 2);
    bf16x8 pf[PFP + 1][MT], qf[PFQ + 1][NT];
    const unsigned lds0 = (unsigned)(uintptr_t)lds;
    auto slot_base = [&](int g) { unsigned b = lds0 + ((g / KST) % NS) * STAGE; if (NS * STAGE > 65536) asm volatile("" : "+s"(b)); return b; };
    auto fetch_p = [&](int g) {
        const unsigned pi = slot_base(g);
#pragma unroll
        for (int m = 0; m < MT; ++m) pf[g % (PFP + 1)][m] = OP::load(pi, pfb[m], g % KST);
    };
    auto fetch_q = [&](int g) {
        const unsigned pi = slot_base(g) + OP::BYTES;
#pragma unroll
        for (int n = 0; n < NT; ++n) qf[g % (PFQ + 1)][n] = OQ::load(pi, qfb[n], g % KST);
    };
#pragma unroll
    for (int g = 0; g < PFM; ++g) { if (g < PFQ) fetch_q(g); if (g < PFP) fetch_p(g); }
#pragma unroll
    for (int g = 0; g < NG; ++g) {
        if ((g + PFM) % KST == 0 && g + PFM < NG) { bare_barrier(); if (((g + PFM) / KST) % 2 == 0 && (g + PFM) / KST < 20) STAMP(ST, 2 + (g + PFM) / KST / 2); }
        if (g + PFQ < NG) fetch_q(g + PFQ);
        if (g + PFP < NG) fetch_p(g + PFP);
#pragma unroll
        for (int m = 0; m < MT; ++m)
#pragma unroll
            for (int n = 0; n < NT; ++n)
                acc[m][n] = SWAP ? MFMA32(qf[g % (PFQ + 1)][n], pf[g % (PFP + 1)][m], acc[m][n]) : MFMA32(pf[g % (PFP + 1)][m], qf[g % (PFQ + 1)][n], acc[m][n]);
        __builtin_amdgcn_sched_barrier(0);
    }
    STAMP(ST, 13);
}

constexpr float QSCALE = 0.22360679774997896f * 1.4426950408889634f;

template <bool ISV>
__device__ __forceinline__ void qkv_tile(LDS_AS char* lds, const u16* x, const u16* w, char* ws, int rt, int ct, int xc, int j, int wid, int lane, Stamps& ST) {
    constexpr int BK = 32, NKT = DM / BK, NS = 6;
    typedef Operand<256, false, BK> OP; typedef Operand<160, true, BK> OQ;
    if (wid >= 8) {
        const int pw = wid - 8;
        const char* xb = (const char*)(x + (size_t)rt * 256 * DM);
        const char* wb = (const char*)(w + ct * 160);
        auto pb = [&](int kt) { return xb + kt * BK * 2; };
        auto qb = [&](int kt) { return wb + (size_t)kt * BK * (3 * DM) * 2; };
        if (pw < 2) gemm_producer<256, false, 160, true, BK, NKT, NS, 4, 3>(lds, pb, DM, qb, 3 * DM, pw, lane, ST, pw * 3);
        else gemm_producer<256, false, 160, true, BK, NKT, NS, 4, 2>(lds, pb, DM, qb, 3 * DM, pw, lane, ST, 6 + (pw - 2) * 2);
        return;
    }
    unsigned pd = 0;
#define QKV_PREF 1
    if (QKV_PREF) {
#pragma unroll
        for (int i = 0; i < 3; ++i) {
            const int L = i * 6144 + j * 512 + wid * 64 + lane;
            if (L < 14720) {
                const int kp = L / 1472, r = L - kp * 1472;
                const char* p = (r < 512) ? (const char*)x + ((xc & 3) * 512 + r) * 1280 + kp * 128
                                          : (const char*)w + (kp * 64 + (r - 512) / 15) * 3840 + (xc >> 2) * 1920 + ((r - 512) % 15) * 128;
                prefetch_line(pd, p);
            }
        }
    }
    const int l31 = lane & 31, hb = lane >> 5;
    f32x16 acc[1][5] = {};
    unsigned pfb[1], qfb[5];
    pfb[0] = OP::frag_base_kc(wid * 32 + l31, hb);
#pragma unroll
    for (int n = 0; n < 5; ++n) qfb[n] = OQ::frag_base_ks(n * 32, lane);
    gemm_consumer<256, false, 160, true, BK, 1, 5, NKT, NS, !ISV, 2, 1>(lds, acc, pfb, qfb, ST);
    prefetch_done(pd);
    const int sec = ct >> 2;
    char* dst = ws + (sec == 0 ? WS_Q : WS_KV + (sec == 2 ? 5120 : 0));
    const int hstride = (sec == 0) ? HEAD_B : 2 * HEAD_B;
    const float sc = (sec == 0) ? QSCALE : 1.0f;
    const int T = rt * 8 + wid;
    const int toff = (sec == 0) ? T * TILE_B : kv_tile_off(T);
#pragma unroll
    for (int n = 0; n < 5; ++n) {
        const int fbase = (ct & 3) * 160 + n * 32;
#pragma unroll
        for (int g = 0; g < 4; ++g) {
            u32x2 v; v[0] = pk2bf(acc[0][n][4 * g] * sc, acc[0][n][4 * g + 1] * sc); v[1] = pk2bf(acc[0][n][4 * g + 2] * sc, acc[0][n][4 * g + 3] * sc);
            unsigned off;
            if constexpr (!ISV) {
                const int g4 = (fbase + 8 * g + 4 * hb) >> 2, head = g4 / 5, gg = g4 - head * 5;
                off = head * hstride + toff + (gg < 4 ? ((gg >> 1) * 32 + l31) * 16 + (gg & 1) * 8 : 1024 + l31 * 8);
            } else {
                const int f = fbase + l31, head = f / DH, d = f - head * DH;
                off = head * hstride + toff + (g >> 1) * 640 + ((g & 1) * 20 + d) * 16 + hb * 8;
            }
            *(u32x2*)(dst + off) = v;
        }
    }
}

struct cf { float re, im; };
__device__ __forceinline__ cf cmul(cf a, cf b) { return {a.re * b.re - a.im * b.im, a.re * b.im + a.im * b.re}; }
__device__ __forceinline__ cf cfma(cf a, cf b, cf c) { return {__builtin_fmaf(a.re, b.re, __builtin_fmaf(-a.im, b.im, c.re)), __builtin_fmaf(a.re, b.im, __builtin_fmaf(a.im, b.re, c.im))}; }
__device__ __forceinline__ cf lam_bar(const u16* lre, const u16* lim, const u16* ldt, int p) {
    const float dt = rbf(expf(bf2f(ldt[p])));
    const float e = expf(dt * bf2f(lre[p])); float sn, cs; sincosf(dt * bf2f(lim[p]), &sn, &cs);
    return {e * cs, e * sn};
}
#define RLX_AGENT __ATOMIC_RELAXED, __HIP_MEMORY_SCOPE_AGENT

__device__ __forceinline__ void s1_task(LDS_AS char* lds, const u16* x, const u16* bre, const u16* bim, const u16* lre, const u16* lim, const u16* ldt,
                                        char* ws, int c, int cg, int wid, int lane, Stamps& ST) {
    constexpr int BK = 64, NKT = DM / BK, NS = 5;
    typedef Operand<128, false, BK> OP;
    LDS_AS f32x2* segE = (LDS_AS f32x2*)(lds + 2 * 32768);
    LDS_AS f32x2* segC = segE + 8 * 64;
    LDS_AS f32x4* pows = (LDS_AS f32x4*)(segC + 8 * 64);
    if (wid >= 8) {
        const int pw = wid - 8;
        GLB_AS unsigned* epoch = (GLB_AS unsigned*)(ws + WS_EPOCH + cg * 256);
        const unsigned tag = __hip_atomic_load(epoch, RLX_AGENT) + 1u;
        const char* xb = (const char*)(x + (size_t)c * 128 * DM);
        const char* qb = (const char*)((pw < 2 ? bre : bim) + (size_t)cg * 64 * DM) - (pw < 2 ? 0 : (size_t)64 * DM * 2);
        gemm_producer<128, false, 128, false, BK, NKT, NS, 3>(lds, [&](int kt) { return xb + kt * BK * 2; }, DM, [&](int kt) { return qb + kt * BK * 2; }, DM, pw, lane, ST);
        if (wid != 8) return;
        const int pc = cg * 64 + lane;
        bare_barrier();
        const f32x4 pw4 = pows[lane];
        const cf m16 = {pw4[0], pw4[1]}, m128 = {pw4[2], pw4[3]};
        cf B = {0.f, 0.f};
#pragma unroll
        for (int s = 0; s < 8; ++s) { f32x2 v = segE[s * 64 + lane]; B = cfma(m16, B, cf{v[0], v[1]}); }
        GLB_AS u64* gr = (GLB_AS u64*)(ws + WS_E);
        if (c < 15) {
            __hip_atomic_store(gr + ((size_t)c * DM + pc) * 2, ((u64)tag << 32) | f2u(B.re), RLX_AGENT);
            __hip_atomic_store(gr + ((size_t)c * DM + pc) * 2 + 1, ((u64)tag << 32) | f2u(B.im), RLX_AGENT);
        }
        u64 ga[15], gb[15];
        for (unsigned spins = 0;; ++spins) {
            bool ok = true;
#pragma unroll
            for (int cp = 0; cp < 15; ++cp) {
                if (cp < c) {
                    ga[cp] = __hip_atomic_load(gr + ((size_t)cp * DM + pc) * 2, RLX_AGENT);
                    gb[cp] = __hip_atomic_load(gr + ((size_t)cp * DM + pc) * 2 + 1, RLX_AGENT);
                } else { ga[cp] = (u64)tag << 32; gb[cp] = (u64)tag << 32; }
            }
#pragma unroll
            for (int cp = 0; cp < 15; ++cp) ok = ok && (unsigned)(ga[cp] >> 32) == tag && (unsigned)(gb[cp] >> 32) == tag;
            if (wave_all(ok) || spins > (1u << 20)) break;
            __builtin_amdgcn_s_sleep(1);
        }
        cf H = {0.f, 0.f};
#pragma unroll
        for (int cp = 0; cp < 15; ++cp) if (cp < c) H = cfma(m128, H, cf{u2f((unsigned)ga[cp]), u2f((unsigned)gb[cp])});
        asm volatile("" ::: "memory");
#pragma unroll
        for (int s = 0; s < 8; ++s) { segC[s * 64 + lane] = f32x2{H.re, H.im}; f32x2 v = segE[s * 64 + lane]; H = cfma(m16, H, cf{v[0], v[1]}); }
        raw_barrier();
        if (c == 15 && lane == 0) __hip_atomic_store(epoch, tag, RLX_AGENT);
        return;
    }
    const int l31 = lane & 31, hb = lane >> 5;
    const int tq = wid & 3, g = wid >> 2;
    const int p = cg * 64 + g * 32 + l31;
    const cf lb = lam_bar(lre, lim, ldt, p);
    cf c2; { const float lr = bf2f(lre[p]), li = bf2f(lim[p]), nr = lb.re - 1.0f, ni = lb.im, den = lr * lr + li * li;
             c2 = {2.0f * ((nr * lr + ni * li) / den), 2.0f * ((ni * lr - nr * li) / den)}; }
    cf m16 = lb;
#pragma unroll
    for (int i = 0; i < 4; ++i) m16 = cmul(m16, m16);
    cf m128 = cmul(m16, m16); m128 = cmul(m128, m128); m128 = cmul(m128, m128);
    f32x16 acc[1][2] = {};
    const int rho = tq * 32 + 16 * ((l31 >> 2) & 1) + (l31 & 3) + 4 * (l31 >> 3);
    unsigned pfb[1] = {OP::frag_base_kc(rho, hb)};
    unsigned qfb[2] = {OP::frag_base_kc(g * 32 + l31, hb), OP::frag_base_kc(64 + g * 32 + l31, hb)};
    gemm_consumer<128, false, 128, false, BK, 1, 2, NKT, NS, false, 2, 2>(lds, acc, pfb, qfb, ST);
    cf E = {0.f, 0.f};
#pragma unroll
    for (int r = 0; r < 16; ++r) E = cfma(lb, E, cf{acc[0][0][r], acc[0][1][r]});
    const int seg = tq * 2 + hb, chl = g * 32 + l31;
    segE[seg * 64 + chl] = f32x2{E.re, E.im};
    if (seg == 0) pows[chl] = f32x4{m16.re, m16.im, m128.re, m128.im};
    raw_barrier();
    STAMP(ST, 14);
    bare_barrier();
    STAMP(ST, 16);
    f32x2 c0 = segC[seg * 64 + chl];
    cf h = {c0[0], c0[1]};
    u16* hT = (u16*)(ws + WS_H);
    const int t0 = c * 128 + tq * 32 + hb * 16;
    u32x4* dre = (u32x4*)(hT + (size_t)p * SEQ + t0); u32x4* dim = (u32x4*)(hT + (size_t)(DM + p) * SEQ + t0);
    unsigned ore[8], oim[8];
#pragma unroll
    for (int r = 0; r < 16; r += 2) {
        h = cfma(lb, h, cf{acc[0][0][r], acc[0][1][r]});         const cf a = cmul(c2, h);
        h = cfma(lb, h, cf{acc[0][0][r + 1], acc[0][1][r + 1]}); const cf b = cmul(c2, h);
        ore[r >> 1] = pk2bf(a.re, b.re); oim[r >> 1] = pk2bf(-a.im, -b.im);
    }
    dre[0] = u32x4{ore[0], ore[1], ore[2], ore[3]}; dre[1] = u32x4{ore[4], ore[5], ore[6], ore[7]};
    dim[0] = u32x4{oim[0], oim[1], oim[2], oim[3]}; dim[1] = u32x4{oim[4], oim[5], oim[6], oim[7]};
}

__global__ void __launch_bounds__(768, 3) k_front(const u16* __restrict__ bx, const u16* __restrict__ wqkv, const u16* __restrict__ ax,
                                                  const u16* __restrict__ bre, const u16* __restrict__ bim, const u16* __restrict__ lre,
                                                  const u16* __restrict__ lim, const u16* __restrict__ ldt, char* __restrict__ ws) {
    extern __shared__ __attribute__((aligned(16))) char smem[];
    LDS_AS char* lds = (LDS_AS char*)smem;
    const int lane = threadIdx.x & 63, wid = __builtin_amdgcn_readfirstlane(threadIdx.x >> 6);
    STAMP_INIT(ST); STAMP(ST, 0);
    const int xc = blockIdx.x & 7;
    if (blockIdx.x < 160) {
        const int j = blockIdx.x >> 3;
        s1_task(lds, ax, bre, bim, lre, lim, ldt, ws, (xc & 3) * 4 + (j & 3), (xc >> 2) * 5 + (j >> 2), wid, lane, ST);
        STAMP(ST, 17); STAMP_FLUSH(ST, ws, 1);
        return;
    }
    const int j = (blockIdx.x - 160) >> 3;
    const int rt = (xc & 3) * 2 + (j & 1), ct = (xc >> 2) * 6 + (j >> 1);
    if (ct < 8) qkv_tile<false>(lds, bx, wqkv, ws, rt, ct, xc, j, wid, lane, ST); else qkv_tile<true>(lds, bx, wqkv, ws, rt, ct, xc, j, wid, lane, ST);
    STAMP(ST, 14); STAMP_FLUSH(ST, ws, 0);
}

__device__ __forceinline__ void s2_task(LDS_AS char* lds, const u16* x, const u16* cre, const u16* cim, const u16* dsk, const char* ws, float* out, int rt, int ct, int koff, int wid, int lane, Stamps& ST) {
    constexpr int BK = 64, NKT = 2 * DM / BK, NS = 6;
    typedef Operand<128, true, BK> OP; typedef Operand<64, false, BK> OQ;
    if (wid >= 4) {
        const char* hb_ = ws + WS_H + (size_t)rt * 128 * 2;
        const char* cr = (const char*)(cre + (size_t)ct * 64 * DM); const char* ci = (const char*)(cim + (size_t)ct * 64 * DM);
        gemm_producer<128, true, 64, false, BK, NKT, NS, 3>(lds, [&](int kt) { return hb_ + (size_t)krot<NKT>(kt, koff) * BK * SEQ * 2; }, SEQ,
            [&](int kt) { const int k = krot<NKT>(kt, koff); return k < NKT / 2 ? cr + k * BK * 2 : ci + (k - NKT / 2) * BK * 2; }, DM, wid - 4, lane, ST);
        return;
    }
    const int l31 = lane & 31, hb = lane >> 5;
    const int wm = wid & 1, wn = wid >> 1;
    f32x16 acc[2][1] = {};
    unsigned pfb[2] = {OP::frag_base_ks(wm * 64, lane), OP::frag_base_ks(wm * 64 + 32, lane)};
    unsigned qfb[1] = {OQ::frag_base_kc(wn * 32 + l31, hb)};
    const int d = ct * 64 + wn * 32 + l31;
    const float ds = bf2f(dsk[d]);
    unsigned xr[2][16];
#pragma unroll
    for (int m = 0; m < 2; ++m)
#pragma unroll
        for (int r = 0; r < 16; ++r) {
            const int t = rt * 128 + wm * 64 + m * 32 + (r & 3) + 8 * (r >> 2) + 4 * hb;
            xr[m][r] = x[(size_t)t * DM + d];
        }
    gemm_consumer<128, true, 64, false, BK, 2, 1, NKT, NS, false, 2, 2>(lds, acc, pfb, qfb, ST);
#pragma unroll
    for (int m = 0; m < 2; ++m)
#pragma unroll
        for (int r = 0; r < 16; ++r) {
            const int t = rt * 128 + wm * 64 + m * 32 + (r & 3) + 8 * (r >> 2) + 4 * hb;
            out[(size_t)t * (2 * DM) + d] = acc[m][0][r] + rbf(bf2f((u16)xr[m][r]) * ds);
        }
}

#define STR2(x) #x
#define XSTR(x) STR2(x)
#define E1(r) "v_exp_f32 v" XSTR(r) ", v" XSTR(r) "\n\t"
#define E2(a, b) E1(a) E1(b)
#define E4(a, b, c, d) E1(a) E1(b) E1(c) E1(d)
#define KOFF(t) "(((" #t ")>>1)&3)*10240+((" #t ")&1)*1280"
#define VOFF(t) "(((" #t ")>>1)&3)*10240+((" #t ")&1)*1280+5120"
#define SMF(O, A, B) "v_smfmac_f32_32x32x32_bf16 " O ", " A ", " B ", v116\n\t"
#define MM1(N, K, Q) "v_mfma_f32_32x32x16_bf16 " N ", " K ", " Q ", 0\n\t"
#define MM2(N, K, Q) "v_mfma_f32_32x32x8_bf16 " N ", " K ", " Q ", " N "\n\t"
#define LOADS(K1d, K2d, V1d, t) \
    "ds_read_b128 " K1d ", v117 offset:" KOFF((t)+2) "\n\tds_read_b64 " K2d ", v118 offset:" KOFF((t)+2) "\n\t" \
    "s_mov_b64 exec, %[vm]\n\tds_read_b128 v[92:95], v119 offset:" VOFF((t)+1) "\n\tds_read_b128 " V1d ", v119 offset:" VOFF((t)+1) "+640\n\ts_mov_b64 exec, -1\n\t"
#define STEP_A(V1P, K1, K2) \
    E2(48, 49) SMF("v[32:47]", V1P, "v[72:79]") E4(50, 51, 52, 53) MM1("v[64:79]", K1, "v[110:113]") E4(54, 55, 56, 57) MM2("v[64:79]", K2, "v[114:115]") E4(58, 59, 60, 61) \
    "s_waitcnt lgkmcnt(0)\n\t" SMF("v[16:31]", "v[92:95]", "v[48:55]") E2(62, 63)
#define STEP_B(V1C, K1, K2, K1d, K2d, V1d, t) \
    E2(64, 65) SMF("v[16:31]", V1C, "v[56:63]") E4(66, 67, 68, 69) MM1("v[48:63]", K1, "v[104:107]") E4(70, 71, 72, 73) MM2("v[48:63]", K2, "v[108:109]") E4(74, 75, 76, 77) \
    SMF("v[32:47]", "v[92:95]", "v[64:71]") E2(78, 79) LOADS(K1d, K2d, V1d, t)
#define PAIRX(t, I0, I1, I3) \
    I0 STEP_A("v[100:103]", "v[80:83]", "v[84:85]") I1 STEP_B("v[96:99]", "v[86:89]", "v[90:91]", "v[80:83]", "v[84:85]", "v[100:103]", t) \
    STEP_A("v[96:99]", "v[86:89]", "v[90:91]") I3 STEP_B("v[100:103]", "v[80:83]", "v[84:85]", "v[86:89]", "v[90:91]", "v[96:99]", (t)+1)
#define GLDS(s) \
    "s_add_u32 m0, %[lw], " #s "*10240\n\ts_nop 0\n\tglobal_load_lds_dwordx4 v120, s[40:41]\n\t" \
    "s_add_u32 m0, %[lw], " #s "*10240+1024\n\ts_nop 0\n\tglobal_load_lds_dword v121, s[40:41]\n\t"
#define ADV "s_add_u32 s40, s40, 10240\n\ts_addc_u32 s41, s41, 0\n\t"
#define UP(t, G) PAIRX(t, "s_setprio 1\n\t", G, "s_setprio 0\n\t")
#define CHUNK(c, s_issue) "s_waitcnt vmcnt(2)\n\ts_barrier\n\t" UP(2*c, GLDS(s_issue) ADV)
#define CHUNK_NOISSUE(c, N) "s_waitcnt vmcnt(" #N ")\n\ts_barrier\n\t" UP(2*c, "")

__device__ __forceinline__ bool attn_row_ok(const f32x16& O, float l) {
    float amax = 0.f;
#pragma unroll
    for (int r = 0; r < 12; ++r) amax = fmaxf(amax, fabsf(O[r]));
    return l >= 5.421010862427522e-20f && l <= 1.8446744073709552e19f && amax < 3.0e38f;
}

__device__ __forceinline__ f32x16 attn_slow(const char* Qt, const char* KVg, int lane) {
    const int l31 = lane & 31, hb = lane >> 5;
    const bf16x8 qf1 = *(const bf16x8*)(Qt + lane * 16);
    u32x2 q2 = *(const u32x2*)(Qt + 1024 + l31 * 8); if (hb) q2 = u32x2{0u, 0u};
    const s16x4 qf2 = __builtin_bit_cast(s16x4, q2);
    const unsigned ones = (l31 == 20) ? 0x3F803F80u : 0u;
    u32x4 vf0 = {ones, ones, ones, ones}, vf1 = vf0;
    f32x16 A = {}; float m = -INFINITY;
    for (int t = 0; t < 64; ++t) {
        const char* kt = KVg + kv_tile_off(t);
        const bf16x8 kf1 = *(const bf16x8*)(kt + lane * 16);
        const s16x4 kf2 = *(const s16x4*)(kt + 1024 + l31 * 8);
        if (l31 < 20) { vf0 = *(const u32x4*)(kt + 5120 + (hb * 20 + l31) * 16); vf1 = *(const u32x4*)(kt + 5120 + 640 + (hb * 20 + l31) * 16); }
        f32x16 s = MFMA32(kf1, qf1, f32x16{});
        s = __builtin_amdgcn_mfma_f32_32x32x8bf16_1k(kf2, qf2, s, 0, 0, 0);
        float tm = s[0];
#pragma unroll
        for (int r = 1; r < 16; ++r) tm = fmaxf(tm, s[r]);
        tm = fmaxf(tm, lane_get(tm, lane ^ 32));
        const float mn = fmaxf(m, tm);
        const float al = __builtin_amdgcn_exp2f(m - mn);
        m = mn;
#pragma unroll
        for (int r = 0; r < 16; ++r) { A[r] *= al; s[r] = __builtin_amdgcn_exp2f(s[r] - mn); }
        A = __builtin_amdgcn_smfmac_f32_32x32x32_bf16(__builtin_bit_cast(bf16x8, vf0), __builtin_bit_cast(bf16x16, __builtin_shufflevector(s, s, 0, 1, 2, 3, 4, 5, 6, 7)), A, 0xDDDDDDDD, 0, 0);
        A = __builtin_amdgcn_smfmac_f32_32x32x32_bf16(__builtin_bit_cast(bf16x8, vf1), __builtin_bit_cast(bf16x16, __builtin_shufflevector(s, s, 8, 9, 10, 11, 12, 13, 14, 15)), A, 0xDDDDDDDD, 0, 0);
    }
    return A;
}

__device__ __forceinline__ void attn_block(LDS_AS char* lds, char* ws, int head, int qt, int wid, int lane0, Stamps& ST) {
    const unsigned lds0 = (unsigned)(uintptr_t)lds;
    const int qg = wid & 3, kh = wid >> 2;
    const char* KVg = ws + WS_KV + (size_t)head * 2 * HEAD_B;
    const char* qp = ws + WS_Q + (size_t)head * HEAD_B + (qt * 8 + qg * 2) * TILE_B;
    f32x16 Oa = {}, Ob = {};
    {
        const int lane = lane0, l31 = lane & 31, hb = lane >> 5;
        u64 gb = (u64)(KVg + wid * 1280);
        const unsigned lw = lds0 + wid * 1280;
        const unsigned ones = (l31 == 20) ? 0x3F803F80u : 0u;
        u32x4 v0 = {ones, ones, ones, ones}, v1a = v0, v1b = v0;
        f32x16 zero16 = {};
        const unsigned ka = lds0 + kh * 2560 + lane * 16, k2a = lds0 + kh * 2560 + 1024 + l31 * 8, va = lds0 + kh * 2560 + (hb * 20 + l31) * 16;
        const unsigned gl = lane * 16, gl4 = 1024 + lane * 4, qo2 = 1024 + l31 * 8;
        const u64 vmask = 0x000FFFFF000FFFFFull, himask = 0xFFFFFFFF00000000ull;
        int cnt = 3; unsigned m0save;
        asm volatile(
            "s_nop 4\n\ts_mov_b32 %[m0s], m0\n\t"
            "global_load_dwordx4 v[104:107], v120, %[qp]\n\t"
            "global_load_dwordx2 v[108:109], %[qo2], %[qp]\n\t"
            "global_load_dwordx4 v[110:113], v120, %[qp] offset:1280\n\t"
            "global_load_dwordx2 v[114:115], %[qo2], %[qp] offset:1280\n\t"
            GLDS(0) ADV GLDS(1) ADV GLDS(2) ADV
            "s_waitcnt vmcnt(2)\n\t"
            "s_mov_b64 exec, %[hm]\n\tv_mov_b32 v108, 0\n\tv_mov_b32 v109, 0\n\tv_mov_b32 v114, 0\n\tv_mov_b32 v115, 0\n\ts_mov_b64 exec, -1\n\t"
            "s_barrier\n\t"
            "ds_read_b128 v[80:83], v117\n\tds_read_b64 v[84:85], v118\n\t"
            "ds_read_b128 v[86:89], v117 offset:1280\n\tds_read_b64 v[90:91], v118 offset:1280\n\t"
            "s_mov_b64 exec, %[vm]\n\tds_read_b128 v[92:95], v119 offset:5120\n\tds_read_b128 v[96:99], v119 offset:5120+640\n\ts_mov_b64 exec, -1\n\t"
            "s_waitcnt lgkmcnt(0)\n\t"
            "v_mfma_f32_32x32x16_bf16 v[48:63], v[80:83], v[104:107], 0\n\t"
            "v_mfma_f32_32x32x8_bf16 v[48:63], v[84:85], v[108:109], v[48:63]\n\t"
            "s_nop 7\n\ts_nop 7\n\t"
            UP(0, GLDS(3) ADV)
            CHUNK(1, 0) CHUNK(2, 1) CHUNK(3, 2)
            "1:\n\t"
            CHUNK(0, 3) CHUNK(1, 0) CHUNK(2, 1) CHUNK(3, 2)
            "s_sub_u32 %[cnt], %[cnt], 1\n\ts_cmp_lg_u32 %[cnt], 1\n\ts_cbranch_scc1 1b\n\t"
            CHUNK(0, 3) CHUNK_NOISSUE(1, 2) CHUNK_NOISSUE(2, 0) CHUNK_NOISSUE(3, 0)
            "s_waitcnt lgkmcnt(0)\n\ts_nop 1\n\t"
            "v_smfmac_f32_32x32x32_bf16 v[32:47], v[100:103], v[72:79], v116\n\t"
            "s_mov_b32 m0, %[m0s]\n\t"
            "s_barrier\n\t"
            "s_nop 15\n\t"
            : "+{v[16:31]}"(Oa), "+{v[32:47]}"(Ob), "+{v[64:79]}"(zero16), "+{v[92:95]}"(v0), "+{v[96:99]}"(v1a), "+{v[100:103]}"(v1b),
              [cnt] "+s"(cnt), "+{s[40:41]}"(gb), [m0s] "=&s"(m0save)
            : "{v116}"(0xDDDDDDDD), "{v117}"(ka), "{v118}"(k2a), "{v119}"(va), "{v120}"(gl), "{v121}"(gl4),
              [vm] "s"(vmask), [hm] "s"(himask), [lw] "s"(lw), [qo2] "v"(qo2), [qp] "s"(qp)
            : "v48", "v49", "v50", "v51", "v52", "v53", "v54", "v55", "v56", "v57", "v58", "v59", "v60", "v61", "v62", "v63",
              "v80", "v81", "v82", "v83", "v84", "v85", "v86", "v87", "v88", "v89", "v90", "v91",
              "v104", "v105", "v106", "v107", "v108", "v109", "v110", "v111", "v112", "v113", "v114", "v115", "scc", "memory");
    }
    STAMP(ST, 2);
    const int lane = fresh_lane(), l31 = lane & 31, hb = lane >> 5;
    LDS_AS f32x4* st = (LDS_AS f32x4*)lds + qg * 6 * 64;
    {
        const f32x16& snd = kh ? Oa : Ob;
#pragma unroll
        for (int g = 0; g < 3; ++g) st[(kh * 3 + g) * 64 + lane] = f32x4{snd[4 * g], snd[4 * g + 1], snd[4 * g + 2], snd[4 * g + 3]};
    }
    raw_barrier();
    f32x16 O = kh ? Ob : Oa;
#pragma unroll
    for (int g = 0; g < 3; ++g) { const f32x4 v = st[((kh ^ 1) * 3 + g) * 64 + lane]; O[4 * g] += v[0]; O[4 * g + 1] += v[1]; O[4 * g + 2] += v[2]; O[4 * g + 3] += v[3]; }
    float l = lane_get(O[8], l31 + 32);
    if (!wave_all(attn_row_ok(O, l))) {
        O = attn_slow(qp + kh * TILE_B, KVg, lane);
        l = lane_get(O[8], l31 + 32);
    }
    const float rl = 1.0f / l;
    u16* op = (u16*)(ws + WS_O) + (size_t)(qt * 256 + qg * 64 + kh * 32 + l31) * DM + head * DH;
#pragma unroll
    for (int g = 0; g < 3; ++g) {
        const int d0 = 8 * g + 4 * hb;
        if (d0 < 20) { u32x2 v; v[0] = pk2bf(O[4 * g] * rl, O[4 * g + 1] * rl); v[1] = pk2bf(O[4 * g + 2] * rl, O[4 * g + 3] * rl); *(u32x2*)(op + d0) = v; }
    }
}

__global__ void __launch_bounds__(576, 3) k_attn(char* __restrict__ ws, const u16* __restrict__ cre, const u16* __restrict__ cim, const u16* __restrict__ wo) {
    extern __shared__ __attribute__((aligned(16))) char smem[];
    const int wid = __builtin_amdgcn_readfirstlane(threadIdx.x >> 6);
    STAMP_INIT(ST); STAMP(ST, 0);
    const int x = blockIdx.x & 7, j = blockIdx.x >> 3, head = 4 * x + (j >> 3), qt = j & 7;
    if (wid == 8) {
        const int G = j * 64 + fresh_lane();
        unsigned d = 0;
#pragma unroll
        for (int b = 0; b < 15; ++b) {
            bare_barrier();
            const int L = b * 2048 + G;
            const char* p;
            if (L < 6400) p = (const char*)cre + L * 128;
            else if (L < 12800) p = (const char*)cim + (L - 6400) * 128;
            else if (L < 19200) p = (const char*)wo + (L - 12800) * 128;
            else p = ws + WS_H + (size_t)((L - 19200) >> 2) * (SEQ * 2) + x * 512 + ((L - 19200) & 3) * 128;
            if (L < 24320) prefetch_line(d, p);
        }
        return;
    }
    attn_block((LDS_AS char*)smem, ws, head, qt, wid, fresh_lane(), ST);
    STAMP(ST, 3); STAMP_FLUSH(ST, ws, 2);
}

__device__ __forceinline__ void oproj_tile(LDS_AS char* lds, const u16* wo, const char* ws, float* out, int rt, int ct, int koff, int wid, int lane, Stamps& ST) {
    constexpr int BK = 64, NKT = DM / BK, NS = 4;
    typedef Operand<128, false, BK> OP; typedef Operand<128, true, BK> OQ;
    if (wid >= 4) {
        const char* ob = ws + WS_O + (size_t)rt * 128 * DM * 2;
        const char* wb = (const char*)(wo + ct * 128);
        gemm_producer<128, false, 128, true, BK, NKT, NS, 2>(lds, [&](int kt) { return ob + krot<NKT>(kt, koff) * BK * 2; }, DM,
            [&](int kt) { return wb + (size_t)krot<NKT>(kt, koff) * BK * DM * 2; }, DM, wid - 4, lane, ST);
        return;
    }
    const int l31 = lane & 31, hb = lane >> 5;
    const int wm = wid & 1, wn = wid >> 1;
    f32x16 acc[2][2] = {};
    unsigned pfb[2], qfb[2];
#pragma unroll
    for (int m = 0; m < 2; ++m) { pfb[m] = OP::frag_base_kc(wm * 64 + m * 32 + l31, hb); qfb[m] = OQ::frag_base_ks(wn * 64 + m * 32, lane); }
    gemm_consumer<128, false, 128, true, BK, 2, 2, NKT, NS, false, 2, 2>(lds, acc, pfb, qfb, ST);
#pragma unroll
    for (int n = 0; n < 2; ++n) {
        const int col = ct * 128 + wn * 64 + n * 32 + l31;
#pragma unroll
        for (int m = 0; m < 2; ++m)
#pragma unroll
            for (int r = 0; r < 16; ++r) {
                const int t = rt * 128 + wm * 64 + m * 32 + (r & 3) + 8 * (r >> 2) + 4 * hb;
                out[(size_t)t * (2 * DM) + DM + col] = rbf(acc[m][n][r]);
            }
    }
}

__global__ void __launch_bounds__(512, 2) k_out(const u16* __restrict__ ax, const u16* __restrict__ cre, const u16* __restrict__ cim, const u16* __restrict__ dsk,
                                                const u16* __restrict__ wo, char* __restrict__ ws, float* __restrict__ out) {
    extern __shared__ __attribute__((aligned(16))) char smem[];
    LDS_AS char* lds = (LDS_AS char*)smem;
    const int lane = threadIdx.x & 63, wid = __builtin_amdgcn_readfirstlane(threadIdx.x >> 6);
    STAMP_INIT(ST); STAMP(ST, 0);
    const int g = blockIdx.x & 7, j = blockIdx.x >> 3;
    if (j < 20) { s2_task(lds, ax, cre, cim, dsk, ws, out, g * 2 + (j & 1), j >> 1, 0, wid, lane, ST); STAMP(ST, 14); STAMP_FLUSH(ST, ws, 3); }
    else { oproj_tile(lds, wo, ws, out, g * 2 + (j & 1), (j - 20) >> 1, 0, wid, lane, ST); STAMP(ST, 14); STAMP_FLUSH(ST, ws, 4); }
}

#if defined(__HIP_DEVICE_COMPILE__) && __HIP_DEVICE_COMPILE__
#pragma clang attribute pop
#endif

extern "C" void kernel_launch(void* const* d_in, const int* in_sizes, int n_in, void* d_out, int out_size, void* d_ws, size_t ws_size, hipStream_t stream) {
    const u16* A_x = (const u16*)d_in[0];
    const u16* L_re = (const u16*)d_in[1];
    const u16* L_im = (const u16*)d_in[2];
    const u16* B_re = (const u16*)d_in[3];
    const u16* B_im = (const u16*)d_in[4];
    const u16* C_re = (const u16*)d_in[5];
    const u16* C_im = (const u16*)d_in[6];
    const u16* logdt = (const u16*)d_in[7];
    const u16* Dsk = (const u16*)d_in[8];
    const u16* B_x = (const u16*)d_in[9];
    const u16* wqkv = (const u16*)d_in[10];
    const u16* wo = (const u16*)d_in[11];
    char* ws = (char*)d_ws;
    float* out = (float*)d_out;
    (void)hipFuncSetAttribute((const void*)k_front, hipFuncAttributeMaxDynamicSharedMemorySize, 163840);
    (void)hipFuncSetAttribute((const void*)k_attn, hipFuncAttributeMaxDynamicSharedMemorySize, 40960);
    (void)hipFuncSetAttribute((const void*)k_out, hipFuncAttributeMaxDynamicSharedMemorySize, 147456);
    k_front<<<256, 768, 163840, stream>>>(B_x, wqkv, A_x, B_re, B_im, L_re, L_im, logdt, ws);
    k_attn<<<256, 576, 40960, stream>>>(ws, C_re, C_im, wo);
    k_out<<<240, 512, 147456, stream>>>(A_x, C_re, C_im, Dsk, wo, ws, out);
}
```
